```python
import torch

def _softmax(x, axis=-1):
    return torch.softmax(x, dim=axis)
def _gelu(x):
    return torch.nn.functional.gelu(x, approximate="tanh")
def _silu(x):
    return torch.nn.functional.silu(x)
def _sigmoid(x):
    return torch.sigmoid(x)
def _layernorm(x, axis=-1, eps=1e-5):
    m = x.mean(dim=axis, keepdim=True)
    v = x.var(dim=axis, keepdim=True, unbiased=False)
    return (x - m) / torch.sqrt(v + eps)
def _scatter_add(tgt, idx, src, axis=0):
    return tgt.clone().index_add_(axis, idx, src)
def _segment_sum(src, seg, n):
    out = torch.zeros((n,) + src.shape[1:], dtype=src.dtype)
    return out.index_add_(0, seg, src)
def _pad(x, pw):
    flat = tuple(v for pair in reversed(pw) for v in pair)
    return torch.nn.functional.pad(x, flat)

kernel_name = 'parallel_sum__spectral_gfnet__glu_gated_bilinear__prompt'

def setup_inputs():

    _w = (lambda a: (lambda t: t.to(torch.bfloat16) if t.is_floating_point() else t)(torch.as_tensor(a)))
    def _A_setup():
        B, S, D = 1, 2048, 448
        Df = D // 2 + 1
        return {
            "x": _w((lambda *s: torch.randn(*s, dtype=torch.bfloat16) * 0.05)(B, S, D)),
            "w_r": _w((lambda *s: torch.randn(*s, dtype=torch.bfloat16) * 0.4)(S, Df) * 0.02),
            "w_i": _w((lambda *s: torch.randn(*s, dtype=torch.bfloat16) * 0.4)(S, Df) * 0.02),
        }
    _A_in = _A_setup()

    def _B_setup():
        B, S, D, F = 1, 2048, 448, 1792
        return {
            "x": _w((lambda *s: torch.randn(*s, dtype=torch.bfloat16) * 0.05)(B, S, D)),
            "w_gate": _w((lambda *s: torch.randn(*s, dtype=torch.bfloat16) * 0.2)(D, F) * (D ** -0.5)),
            "w_up": _w((lambda *s: torch.randn(*s, dtype=torch.bfloat16) * 0.2)(D, F) * (D ** -0.5)),
            "w_down": _w((lambda *s: torch.randn(*s, dtype=torch.bfloat16) * 0.2)(F, D) * (F ** -0.5)),
        }
    _B_in = _B_setup()

    return {
        "A_x": _A_in["x"],
        "A_w_r": _A_in["w_r"],
        "A_w_i": _A_in["w_i"],
        "B_x": _B_in["x"],
        "B_w_gate": _B_in["w_gate"],
        "B_w_up": _B_in["w_up"],
        "B_w_down": _B_in["w_down"],
    }

def reference(A_x, A_w_r, A_w_i, B_x, B_w_gate, B_w_up, B_w_down):
    def _A_ref(x, w_r, w_i):
        B, S, D = x.shape
        X = (lambda x, axes=(-2,-1): torch.fft.rfft2(x.float(), dim=axes))(x, axes=(-2, -1))
        W = w_r + 1j * w_i
        X = X * W
        y = (lambda x, s, axes=(-2,-1): torch.fft.irfft2(x, s=s, dim=axes).to(torch.bfloat16))(X, (S, D), axes=(-2, -1))
        return y

    def _B_ref(x, w_gate, w_up, w_down):
        g = torch.einsum("bsd,df->bsf", x, w_gate)
        u = torch.einsum("bsd,df->bsf", x, w_up)
        h = g * u
        return torch.einsum("bsf,fd->bsd", h, w_down)

    x = A_x
    xn = _layernorm(x, axis=-1)
    return x + _A_ref(xn, A_w_r, A_w_i) + _B_ref(xn, B_w_gate, B_w_up, B_w_down)
```

```cpp
#define T0 20
#include <hip/hip_runtime.h>
#include <stdint.h>
#include <type_traits>

typedef unsigned u32;
typedef unsigned long long u64;
typedef u32 u32x2 __attribute__((ext_vector_type(2)));
typedef u32 u32x4 __attribute__((ext_vector_type(4)));
typedef float f32x2 __attribute__((ext_vector_type(2)));
typedef float f32x4 __attribute__((ext_vector_type(4)));
typedef __bf16 bf16x2 __attribute__((ext_vector_type(2)));
typedef __bf16 bf16x4 __attribute__((ext_vector_type(4)));
typedef __bf16 bf16x8 __attribute__((ext_vector_type(8)));
typedef __amdgpu_buffer_rsrc_t rsrc_t;
#define LAS __attribute__((address_space(3)))
#define GAS __attribute__((address_space(1)))
#define DI __device__ __forceinline__
#define SB() __builtin_amdgcn_sched_barrier(0)

constexpr u32 WS_FLAG_A = 4096, WS_FLAG_B = WS_FLAG_A + 32768, WS_FLAG_W = WS_FLAG_B + 32768, WS_FLAG_V = WS_FLAG_W + 4096;
constexpr u32 WS_Z = 131072, WS_WT = WS_Z + 448 * 4096, WS_H = WS_WT + 225 * 8192, WS_V = WS_H + 32 * 56 * 4096;
constexpr u32 WS_ST = WS_V + 32 * 448 * 128, WS_CS = WS_ST + 2048 * 16, WS_R2 = WS_CS + 3584 * 16, WS_END = WS_R2 + 2048 * 4;
constexpr u32 WS_PROF = 16u << 20;
constexpr u32 SLOT = 16384, L_F = 8 * SLOT, L_XCH = 9 * SLOT, L_SYNC = L_XCH + 12288, LDS_BYTES = 163840;
constexpr u32 L_ZERO = L_SYNC + 128, L_NM = L_SYNC + 256, L_RI = L_SYNC + 768, L_CS = L_SYNC + 1280, L_CSB = L_XCH + 8192, L_JUNK = L_SYNC + 3840;
constexpr u32 L_HT = 5 * SLOT;
constexpr u32 Y_FREE = L_SYNC, Y_TAB = L_SYNC + 8, Y_HA = Y_TAB + 8, Y_HB = Y_HA + 4, Y_ZI = Y_HB + 4, Y_ZD = Y_ZI + 4, Y_DL = Y_ZD + 4,
              Y_XCH = Y_DL + 4, Y_VD = Y_XCH + 4, Y_VS = Y_VD + 4, Y_VP = Y_VS + 4, Y_VL = Y_VP + 4, Y_V0 = Y_VL + 8, Y_G0 = Y_V0 + 16, Y_G1 = Y_G0 + 8, Y_E = Y_G1 + 8, Y_V1 = Y_E + 16;
static_assert(Y_V1 < L_ZERO, "");
constexpr u32 X_BYTES = 2048 * 448 * 2, W_BYTES = 448 * 1792 * 2, FILT_BYTES = 2048 * 225 * 2;
constexpr u32 CODE_BYTES = 36864;
constexpr u32 SPIN_CAP = 1u << 16;

#define STAMP(i) do {} while (0)
#define CSTAMP(i) do {} while (0)
#define CNT(i, n) do { (void)(n); } while (0)
#define STAMP_INIT() do {} while (0)
#define STAMP_DUMP() do {} while (0)

#define CK2(i) do {} while (0)
DI u32 pk(float a, float b) { f32x2 v = {a, b}; return __builtin_bit_cast(u32, __builtin_convertvector(v, bf16x2)); }
DI float bflo(u32 v) { return __uint_as_float(v << 16); }
DI float bfhi(u32 v) { return __uint_as_float(v & 0xffff0000u); }
DI float rbf(float a) { return bflo(pk(a, a)); }
DI f32x4 mfma16(u32x4 a, u32x4 b, f32x4 c) { return __builtin_amdgcn_mfma_f32_16x16x32_bf16(__builtin_bit_cast(bf16x8, a), __builtin_bit_cast(bf16x8, b), c, 0, 0, 0); }
DI void mma(f32x4& acc, u32x4 a, u32x4 b) { asm volatile("v_mfma_f32_16x16x32_bf16 %0, %1, %2, %0" : "+v"(acc) : "v"(a), "v"(b)); }
DI void mma0(f32x4& acc, u32x4 a, u32x4 b) { asm volatile("v_mfma_f32_16x16x32_bf16 %0, %1, %2, 0" : "=&v"(acc) : "v"(a), "v"(b)); }
DI void mmak16(f32x4& acc, u32x2 a, u32x2 b) { asm volatile("v_mfma_f32_16x16x16_bf16 %0, %1, %2, %0" : "+v"(acc) : "v"(a), "v"(b)); }
DI float mul1(float a, float b) { float r; asm("v_mul_f32 %0, %1, %2" : "=v"(r) : "v"(a), "v"(b)); return r; }
DI void mma_pad() { asm volatile("s_nop 7"); }
DI u32x2 lds_tr1(LAS char* L, u32 a) { return __builtin_bit_cast(u32x2, __builtin_amdgcn_ds_read_tr16_b64_v4bf16((LAS bf16x4*)(L + a))); }
DI u32x4 cat(u32x2 a, u32x2 b) { return u32x4{a[0], a[1], b[0], b[1]}; }
typedef short s16x4 __attribute__((ext_vector_type(4)));
DI f32x4 mfma16k16(u32x2 a, u32x2 b, f32x4 c) { return __builtin_amdgcn_mfma_f32_16x16x16bf16_1k(__builtin_bit_cast(s16x4, a), __builtin_bit_cast(s16x4, b), c, 0, 0, 0); }
DI float dot2(u32 a, u32 b, float c) { return __builtin_amdgcn_fdot2_f32_bf16(__builtin_bit_cast(bf16x2, a), __builtin_bit_cast(bf16x2, b), c, false); }
DI float cosr(float x) { return __builtin_amdgcn_cosf(x); }
DI float sinr(float x) { return __builtin_amdgcn_sinf(x); }
DI u32x4 mk_srd(const void* p, u32 bytes) { u64 a = (u64)p; return u32x4{(u32)a, (u32)(a >> 32) & 0xffffu, bytes, 0x00020000u}; }
template <int N> DI void wait_vm() { asm volatile("s_waitcnt vmcnt(%0)" ::"n"(N) : "memory"); }
DI void wait_lgkm0() { asm volatile("s_waitcnt lgkmcnt(0)" ::: "memory"); }
DI void cbar() { asm volatile("" ::: "memory"); }
DI void barrier() { cbar(); __builtin_amdgcn_s_barrier(); cbar(); }
template <class T> DI void pin(T& v) { asm volatile("" : "+v"(v)); }
DI float pf(float v) { asm volatile("" : "+v"(v)); return v; }
DI u32 pv(u32 v) { asm volatile("" : "+v"(v)); return v; }
template <u32 LO, u32 SO> DI void glds1(u32 v0, u32x4 r0, u32 sbase, u32 lbase) {
  u32 t;
  asm volatile("s_add_u32 m0, %4, %5\n\ts_add_u32 %0, %3, %6\n\tbuffer_load_dwordx4 %1, %2, %0 offen lds"
               : "=&s"(t) : "v"(v0), "s"(r0), "s"(sbase), "s"(lbase), "n"(LO), "n"(SO) : "memory", "scc");
}
template <u32 LO, u32 SO> DI void glds1_sc1(u32 v0, u32x4 r0, u32 sbase, u32 lbase) {
  u32 t;
  asm volatile("s_add_u32 m0, %4, %5\n\ts_add_u32 %0, %3, %6\n\tbuffer_load_dwordx4 %1, %2, %0 offen sc1 lds"
               : "=&s"(t) : "v"(v0), "s"(r0), "s"(sbase), "s"(lbase), "n"(LO), "n"(SO) : "memory", "scc");
}
template <u32 LO, u32 SO> DI void glds2(u32 v0, u32x4 r0, u32 v1, u32x4 r1, u32 sbase, u32 lbase) {
  u32 t;
  asm volatile("s_add_u32 m0, %6, %7\n\ts_add_u32 %0, %5, %8\n\tbuffer_load_dwordx4 %1, %2, %0 offen lds\n\tbuffer_load_dwordx4 %3, %4, %0 offen offset:1024 lds"
               : "=&s"(t) : "v"(v0), "s"(r0), "v"(v1), "s"(r1), "s"(sbase), "s"(lbase), "n"(LO), "n"(SO) : "memory", "scc");
}
template <u32 LO, u32 SO> DI void glds4(const u32 (&v)[4], u32x4 r, u32 sbase, u32 lbase) {
  u32 t;
  asm volatile("s_add_u32 m0, %7, %8\n\ts_add_u32 %0, %6, %9\n\tbuffer_load_dwordx4 %1, %5, %0 offen lds\n\tbuffer_load_dwordx4 %2, %5, %0 offen offset:1024 lds\n\t"
               "buffer_load_dwordx4 %3, %5, %0 offen offset:2048 lds\n\tbuffer_load_dwordx4 %4, %5, %0 offen offset:3072 lds"
               : "=&s"(t) : "v"(v[0]), "v"(v[1]), "v"(v[2]), "v"(v[3]), "s"(r), "s"(sbase), "s"(lbase), "n"(LO), "n"(SO) : "memory", "scc");
}
template <u32 LO, u32 SO> DI void glds4w(u32 v0, u32 v1, u32x4 r0, u32x4 r1, u32 sbase, u32 lbase) {
  u32 t;
  asm volatile("s_add_u32 m0, %6, %7\n\ts_add_u32 %0, %5, %8\n\tbuffer_load_dwordx4 %1, %3, %0 offen lds\n\tbuffer_load_dwordx4 %1, %4, %0 offen offset:1024 lds\n\t"
               "buffer_load_dwordx4 %2, %3, %0 offen offset:2048 lds\n\tbuffer_load_dwordx4 %2, %4, %0 offen offset:3072 lds"
               : "=&s"(t) : "v"(v0), "v"(v1), "s"(r0), "s"(r1), "s"(sbase), "s"(lbase), "n"(LO), "n"(SO) : "memory", "scc");
}
template <u32 O> DI void ld128(u32x4& d, u32 v, u32x4 r, u32 s) {
  asm volatile("buffer_load_dwordx4 %0, %1, %2, %3 offen offset:%4 sc1" : "=v"(d) : "v"(v), "s"(r), "s"(s), "n"(O) : "memory");
}
template <u32 SO> DI void ld128x4(u32x4& d0, u32x4& d1, u32x4& d2, u32x4& d3, u32 v, u32x4 r, u32 s) {
  u32 t;
  asm volatile("s_add_u32 %4, %7, %8\n\tbuffer_load_dwordx4 %0, %5, %6, %4 offen sc1\n\tbuffer_load_dwordx4 %1, %5, %6, %4 offen offset:1024 sc1\n\t"
               "buffer_load_dwordx4 %2, %5, %6, %4 offen offset:2048 sc1\n\tbuffer_load_dwordx4 %3, %5, %6, %4 offen offset:3072 sc1"
               : "=&v"(d0), "=&v"(d1), "=&v"(d2), "=&v"(d3), "=&s"(t) : "v"(v), "s"(r), "s"(s), "n"(SO) : "memory", "scc");
}
#define OUTPOL " sc1"
template <u32 O> DI void st128(u32x4 d, u32 v, u32x4 r, u32 s) {
  asm volatile("buffer_store_dwordx4 %0, %1, %2, %3 offen offset:%4 sc1\n\ts_nop 1" :: "v"(d), "v"(v), "s"(r), "s"(s), "n"(O) : "memory");
}
DI void st128_plain(u32x4 d, u32 v, u32x4 r, u32 s) {
  asm volatile("buffer_store_dwordx4 %0, %1, %2, %3 offen" OUTPOL "\n\ts_nop 1" :: "v"(d), "v"(v), "s"(r), "s"(s) : "memory");
}
DI void ld32(float& d, u32 v, u32x4 r, u32 s) { asm volatile("buffer_load_dword %0, %1, %2, %3 offen sc1" : "=v"(d) : "v"(v), "s"(r), "s"(s) : "memory"); }
DI void st32(float d, u32 v, u32x4 r, u32 s) { asm volatile("buffer_store_dword %0, %1, %2, %3 offen sc1" :: "v"(d), "v"(v), "s"(r), "s"(s) : "memory"); }
DI void ld64(u32x2& d, u32 v, u32x4 r) { asm volatile("buffer_load_dwordx2 %0, %1, %2, 0 offen sc1" : "=v"(d) : "v"(v), "s"(r) : "memory"); }
DI u32x4 lds128(LAS char* L, u32 a) { return *(LAS u32x4*)(L + a); }
DI u32x4 lds_tr(LAS char* L, u32 a0, u32 a1) {
  bf16x4 lo = __builtin_amdgcn_ds_read_tr16_b64_v4bf16((LAS bf16x4*)(L + a0));
  bf16x4 hi = __builtin_amdgcn_ds_read_tr16_b64_v4bf16((LAS bf16x4*)(L + a1));
  return __builtin_bit_cast(u32x4, __builtin_shufflevector(lo, hi, 0, 1, 2, 3, 4, 5, 6, 7));
}
DI u32 lds_arrive(u32 a) {
  u32 old, r;
  asm volatile("s_mov_b64 exec, 1\n\tds_add_rtn_u32 %0, %2, %3\n\ts_mov_b64 exec, -1\n\ts_waitcnt lgkmcnt(0)\n\tv_readfirstlane_b32 %1, %0"
               : "=&v"(old), "=s"(r) : "v"(a), "v"(1u) : "memory");
  return r;
}
DI void lds_post(u32 a) { asm volatile("s_mov_b64 exec, 1\n\tds_add_u32 %0, %1\n\ts_mov_b64 exec, -1" :: "v"(a), "v"(1u) : "memory"); }
template <int SL = 0> DI void lds_wait(u32 a, u32 want) {
#pragma unroll 1
  for (u32 s = 0; s < (1u << 22); ++s) {
    u32 v, r;
    asm volatile("ds_read_b32 %0, %2\n\ts_waitcnt lgkmcnt(0)\n\tv_readfirstlane_b32 %1, %0" : "=&v"(v), "=s"(r) : "v"(a) : "memory");
    if (r >= want) break;
    if (SL) __builtin_amdgcn_s_sleep(SL);
  }
}
DI void lds_sync(u32 a, u32 n) { if (lds_arrive(a) != n - 1) lds_wait(a, n); }
#define PSLEEP 2
#define VSLEEP 8
template <int SL = PSLEEP> DI u32 poll_flags(u32x2 v, u32 off, u32x4 sws, u32 tlo, u32 thi) {
  u32 s = 0;
#pragma unroll 1
  for (; s < SPIN_CAP; ++s) {
    if (__builtin_amdgcn_ballot_w64(((v[0] ^ tlo) | (v[1] ^ thi)) != 0) == 0) break;
    if (s) __builtin_amdgcn_s_sleep(SL);
    ld64(v, off, sws);
    wait_vm<0>();
    pin(v);
  }
  cbar();
  return s;
}
DI void set_flag(GAS char* ws, u32 off, u32 tlo, u32 thi) { __hip_atomic_store((GAS u64*)(ws + off), (u64)tlo | ((u64)thi << 32), __ATOMIC_RELAXED, __HIP_MEMORY_SCOPE_AGENT); }
DI u32x4 mk_gran(u32 a_hi16, u32 b_hi16, u32 tlo, u32 thi) { return u32x4{(a_hi16 & 0xffff0000u) | (tlo & 0xffffu), thi, (b_hi16 & 0xffff0000u) | (tlo & 0xffffu), thi}; }
DI u32 gran_lo(u32x4 g, u32 tlo) { return (g[0] ^ tlo) | (g[2] ^ tlo); }
DI u32 gran_hi(u32x4 g, u32 thi) { return (g[1] ^ thi) | (g[3] ^ thi); }
DI bool all_zero(u32 v) { return __builtin_amdgcn_ballot_w64(v != 0) == 0; }
template <class T> DI void pins(T& v) { asm volatile("" : "+s"(v)); }

template <int N> DI void wait_tag(u32x2 ep, u32& tlo, u32& thi) {
  u32 a, b;
  asm volatile("s_waitcnt vmcnt(%4)\n\tv_readfirstlane_b32 %0, %2\n\tv_readfirstlane_b32 %1, %3" : "=s"(a), "=s"(b) : "v"(ep[0]), "v"(ep[1]), "n"(N) : "memory");
  const u64 tg = ((u64)a | ((u64)b << 32)) * 6364136223846793005ull + 1442695040888963407ull;
  tlo = (u32)tg; thi = (u32)(tg >> 32);
}

#define GP(x) pf(x)
#define EPI0 4
#define TMASK 8
#define PSL 2
#define WSOFF 0
#define LSL 1
#define GPRIO 2
#define G2PRIO 0
#define TPRIO 2
#define VBASE WS_V
#define HPOL " sc1"
#define VPD 3
#define VC0 8
#define FLATE 16
#define FMID (FLATE + 6)
#define GLATE 1
#define GPRIO2 2
#define PGN 0
#define SPRIO 2
#define CPRIO 1
#define MPRIO 1
#define FPRIO 2
#define MPER 2
#define MK0 2

struct Ctx {
  LAS char* L;
  GAS char* ws;
  u32x4 sx, sg, su, sd, sws;
  int w, lane, mi, ni, nt, tau, kcol;
  u32 vx[4], vw[2], vwd[4];
  u32 sw, sdn, lb;
  u32 tlo, thi;
  u32 l16;
  u32 rot, rot1;
};
template <int Y> DI u32 rot7(u32 rot) { const u32 v = rot + Y; return v >= 7 ? v - 7 : v; }
constexpr int w_slot(int J) { return J < 9 ? J % 4 : J == 9 ? 4 : (J - 1) % 4; }
constexpr int wd_slot(int y) { return y <= 3 ? 8 - y : y <= 8 ? 12 - y : y <= 12 ? (y - 8) % 4 : 8; }
DI u32 wd_src(int lane, int i) { return (lane >> 3) * 896 + i * (7168 - 1024) + ((lane & 7) ^ (2 * (lane >> 4))) * 16; }
template <int ID> DI void stage_in(const Ctx& c) {
  if constexpr (ID < 10) {
    glds4<(4 + ID % 4) * SLOT, 0>(c.vx, c.sx, rot7<ID>(c.rot1) * 128, c.lb);
  } else if constexpr (ID < 30) {
    constexpr int J = ID - 10;
    glds4w<w_slot(J) * SLOT, (J / 7) * 128>(c.vw[0], c.vw[1], c.sg, c.su, c.sw + rot7<J % 7>(c.rot1) * (64 * 3584), c.lb);
  } else {
    constexpr int y = ID - 30;
    glds4<wd_slot(y) * SLOT, 64 * (y / 7) * 896>(c.vwd, c.sd, c.sdn + rot7<y % 7>(c.rot) * (256 * 896), c.lb);
  }
}
template <u32 LO, u32 SO> DI u32 glds_open(u32 sbase, u32 lbase) {
  u32 t;
  asm volatile("s_add_u32 m0, %2, %3\n\ts_add_u32 %0, %1, %4" : "=&s"(t) : "s"(sbase), "s"(lbase), "n"(LO), "n"(SO) : "memory", "scc");
  return t;
}
template <u32 O> DI void glds_one(u32 v, u32x4 r, u32 t) { asm volatile("buffer_load_dwordx4 %0, %1, %2 offen offset:%3 lds" :: "v"(v), "s"(r), "s"(t), "n"(O) : "memory"); }
template <int ID> DI u32 stage_open(const Ctx& c) {
  if constexpr (ID < 10) return glds_open<(4 + ID % 4) * SLOT, 0>(rot7<ID>(c.rot1) * 128, c.lb);
  else if constexpr (ID < 30) return glds_open<w_slot(ID - 10) * SLOT, ((ID - 10) / 7) * 128>(c.sw + rot7<(ID - 10) % 7>(c.rot1) * (64 * 3584), c.lb);
  else return glds_open<wd_slot(ID - 30) * SLOT, 64 * ((ID - 30) / 7) * 896>(c.sdn + rot7<(ID - 30) % 7>(c.rot) * (256 * 896), c.lb);
}
template <int ID, int K> DI void stage_one(const Ctx& c, u32 t) {
  if constexpr (ID < 10) glds_one<K * 1024>(c.vx[K], c.sx, t);
  else if constexpr (ID < 30) glds_one<K * 1024>(c.vw[K >> 1], (K & 1) ? c.su : c.sg, t);
  else glds_one<K * 1024>(c.vwd[K], c.sd, t);
}
DI void touch(u32 v, u32x4 r, u32 so) {
  asm volatile("s_mov_b32 m0, %3\n\ts_nop 0\n\tbuffer_load_dword %0, %1, %2 offen lds" :: "v"(v), "s"(r), "s"(so), "n"(L_JUNK) : "memory");
}
DI u32 lds_peek(LAS char* L, u32 a) { return *(volatile LAS u32*)(L + a); }
DI void lds_check(u32 v, u32 a, u32 want) { if (__builtin_amdgcn_readfirstlane(v) < want) lds_wait(a, want); cbar(); }
#define NDS 2

DI int pi_(int T, int r) { return 16 * (r >> 3) + 8 * T + (r & 7); }
struct FftC { u32x4 c64[2], s64[2], n64[2], c32[2], s32[2], n32[2]; f32x4 t1c[2], t1s[2], t2c[2], t2s[2]; };
struct FftIn { u32x4 zr[2][2], zi[2][2], wt[2]; };
struct FftL { float f64, f32_, g4, gp, f1, f2[2], gq; u32 sg; };
DI void fft_lane(FftL& l, int q, int lane) {
  const int r = lane & 15, g = lane >> 4;
  l.f64 = (float)(16 * q + r) * (1.f / 64.f); l.f32_ = (float)pi_(0, r) * (1.f / 32.f);
  l.g4 = (float)(4 * g); l.gp = (float)pi_(0, 4 * g); l.gq = (float)(16 * q + 4 * g);
  l.sg = (r & 1) ? 0x80008000u : 0u;
}
DI u32 lohi(u32 lo, u32 hi) { return __builtin_amdgcn_perm(hi, lo, 0x07060100u); }
template <int CH> DI void fft_gen(FftC& k, const FftL& l) {
  constexpr u32 NEG = 0x80008000u;
  constexpr int j = (CH >> 1) & 3;
  if constexpr (CH < 8 && !(CH & 1)) {
    const float p0 = l.f64 * (l.g4 + (float)(16 * (j >> 1) + 2 * (j & 1))), p1 = p0 + l.f64;
    const u32 cc = pv(pk(cosr(p0), cosr(p1))), ss = pv(pk(sinr(p0), sinr(p1))), nn = pv(ss ^ NEG);
    k.c64[0][j] = cc; k.s64[0][j] = ss; k.n64[0][j] = nn;
    k.c64[1][j] = pv(cc ^ l.sg); k.s64[1][j] = pv(ss ^ l.sg); k.n64[1][j] = pv(nn ^ l.sg);
  } else if constexpr (CH < 8) {
    const float q0 = l.f32_ * (l.gp + (float)(8 * (j >> 1) + 2 * (j & 1))), q1 = q0 + l.f32_;
    const u32 c = pv(pk(cosr(q0), cosr(q1))), sn = pv(pk(sinr(q0), sinr(q1))), n = pv(sn ^ NEG);
    k.c32[0][j] = c; k.s32[0][j] = sn; k.n32[0][j] = n;
    const u32 sc = pv(lohi(sn, c));
    if constexpr (j & 1) { k.c32[1][j] = pv(lohi(c, n) ^ NEG); k.s32[1][j] = pv(sc ^ NEG); k.n32[1][j] = sc; }
    else { k.c32[1][j] = pv(lohi(c, n)); k.s32[1][j] = sc; k.n32[1][j] = pv(sc ^ NEG); }
  } else {
    constexpr int a = CH & 1;
    const float a1 = (l.gp + (float)(8 * a + j)) * (l.f64 * (1.f / 32.f)), a2 = (l.gq + (float)j) * ((l.f32_ + 0.25f * a) * (1.f / 64.f));
    k.t1c[a][j] = pf(cosr(a1)); k.t1s[a][j] = pf(sinr(a1)); k.t2c[a][j] = pf(cosr(a2)); k.t2s[a][j] = pf(sinr(a2));
  }
}
template <int CH> DI void fft_gen_all(FftC& k, const FftL& l) { fft_gen<CH>(k, l); if constexpr (CH < 15) fft_gen_all<CH + 1>(k, l); }
template <int A, int B> DI void fft_gen_rng(FftC& k, const FftL& l) { if constexpr (A < B) { fft_gen<A>(k, l); fft_gen_rng<A + 1, B>(k, l); } }
DI void fft_load(FftIn& in, u32x4 sws, int q, int lane, int k, int kw) {
  const int r = lane & 15, g = lane >> 4;
  const u32 zv = g * 512 + (r >> 3) * 256 + (r & 7) * 16, zs = WS_Z + 2 * k * 4096, wsoff = WS_WT + kw * 8192 + q * 2048;
  ld128<0>(in.zr[0][0], zv, sws, zs); ld128<0>(in.zi[0][0], zv, sws, zs + 4096);
  ld128<2048>(in.zr[0][1], zv, sws, zs); ld128<2048>(in.zi[0][1], zv, sws, zs + 4096);
  ld128<128>(in.zr[1][0], zv, sws, zs); ld128<128>(in.zi[1][0], zv, sws, zs + 4096);
  ld128<2176>(in.zr[1][1], zv, sws, zs); ld128<2176>(in.zi[1][1], zv, sws, zs + 4096);
  ld128<0>(in.wt[0], lane * 16, sws, wsoff); ld128<1024>(in.wt[1], lane * 16, sws, wsoff);
}
DI void fft_pin(FftIn& in) {
#pragma unroll
  for (int i = 0; i < 4; ++i) { pin(in.zr[i >> 1][i & 1]); pin(in.zi[i >> 1][i & 1]); }
  pin(in.wt[0]); pin(in.wt[1]);
}
template <int MODE> DI void fft_run(const FftC& k, const FftIn& in, LAS char* L, u32 xch, u32 syncw, u32x4 rws, int q, int grp, int lane, int kcol) {
  const int r = lane & 15, g = lane >> 4;
  const f32x4 z4 = {0.f, 0.f, 0.f, 0.f};
  f32x4 ar[2], ai[2];
  u32x4 pr, pi;
#pragma unroll
  for (int T = 0; T < 2; ++T) { ar[T] = mfma16(in.zr[T][0], k.c64[0], z4); ai[T] = mfma16(in.zi[T][0], k.c64[0], z4); }
#pragma unroll
  for (int T = 0; T < 2; ++T) { ar[T] = mfma16(in.zi[T][0], k.s64[0], ar[T]); ai[T] = mfma16(in.zr[T][0], k.n64[0], ai[T]); }
#pragma unroll
  for (int T = 0; T < 2; ++T) {
    ar[T] = mfma16(in.zr[T][1], k.c64[1], ar[T]); ai[T] = mfma16(in.zi[T][1], k.c64[1], ai[T]);
    ar[T] = mfma16(in.zi[T][1], k.s64[1], ar[T]); ai[T] = mfma16(in.zr[T][1], k.n64[1], ai[T]);
  }
  f32x4 wr[2], wi[2];
#pragma unroll
  for (int U = 0; U < 2; ++U)
#pragma unroll
    for (int j = 0; j < 4; ++j) { wr[U][j] = bflo(in.wt[U][j]); wi[U][j] = bfhi(in.wt[U][j]); }
  const u32 wa = xch + (MODE ? grp * 8192 : 0) + (q >> 1) * 4096 + lane * 16 + (q & 1) * 8, ra = xch + lane * 16;
  const u32 vv = (2 * (2 * g + (r >> 3))) * (448 * 128) + (r & 7) * 16;
  SB();
#pragma unroll
  for (int T = 0; T < 2; ++T) {
    const f32x4 xr = ar[T] * k.t1c[T] + ai[T] * k.t1s[T], xi = ai[T] * k.t1c[T] - ar[T] * k.t1s[T];
    pr[2 * T] = pk(xr[0], xr[1]); pr[2 * T + 1] = pk(xr[2], xr[3]); pi[2 * T] = pk(xi[0], xi[1]); pi[2 * T + 1] = pk(xi[2], xi[3]);
  }
  SB();
#pragma unroll
  for (int U = 0; U < 2; ++U) {
    ar[U] = mfma16(k.c32[U], pr, z4); ai[U] = mfma16(k.c32[U], pi, z4);
    ar[U] = mfma16(k.s32[U], pi, ar[U]); ai[U] = mfma16(k.n32[U], pr, ai[U]);
  }
  SB();
#pragma unroll
  for (int U = 0; U < 2; ++U) {
    const f32x4 xr = ar[U] * wr[U] - ai[U] * wi[U], xi = ar[U] * wi[U] + ai[U] * wr[U];
    pr[2 * U] = pk(xr[0], xr[1]); pr[2 * U + 1] = pk(xr[2], xr[3]); pi[2 * U] = pk(xi[0], xi[1]); pi[2 * U + 1] = pk(xi[2], xi[3]);
  }
  SB();
#pragma unroll
  for (int T = 0; T < 2; ++T) {
    ar[T] = mfma16(pr, k.c32[T], z4); ai[T] = mfma16(pi, k.c32[T], z4);
    ar[T] = mfma16(pi, k.n32[T], ar[T]); ai[T] = mfma16(pr, k.s32[T], ai[T]);
  }
  SB();
#pragma unroll
  for (int T = 0; T < 2; ++T) {
    const f32x4 xr = ar[T] * k.t2c[T] - ai[T] * k.t2s[T], xi = ai[T] * k.t2c[T] + ar[T] * k.t2s[T];
    *(LAS u32x2*)(L + wa + T * 2048) = u32x2{pk(xr[0], xr[1]), pk(xr[2], xr[3])};
    *(LAS u32x2*)(L + wa + T * 2048 + 1024) = u32x2{pk(xi[0], xi[1]), pk(xi[2], xi[3])};
  }
  cbar();
  if (MODE && grp) { lds_post(syncw); return; }
  lds_sync(syncw, MODE ? 8 : 4);
  u32x4 br[2][2], bi[2][2], br2[2][2], bi2[2][2];
#pragma unroll
  for (int i = 0; i < 4; ++i) {
    br[i >> 1][i & 1] = lds128(L, ra + i * 2048); bi[i >> 1][i & 1] = lds128(L, ra + i * 2048 + 1024);
    br2[i >> 1][i & 1] = MODE ? lds128(L, ra + 8192 + i * 2048) : br[i >> 1][i & 1];
    bi2[i >> 1][i & 1] = MODE ? lds128(L, ra + 8192 + i * 2048 + 1024) : bi[i >> 1][i & 1];
  }
  SB();
#pragma unroll
  for (int Pq = 0; Pq < 2; ++Pq)
#pragma unroll
    for (int T = 0; T < 2; ++T) {
      ar[T] = mfma16(k.c64[Pq], br[Pq][T], Pq ? ar[T] : z4); ai[T] = mfma16(k.c64[Pq], bi2[Pq][T], Pq ? ai[T] : z4);
      ar[T] = mfma16(k.n64[Pq], bi[Pq][T], ar[T]); ai[T] = mfma16(k.s64[Pq], br2[Pq][T], ai[T]);
    }
  SB();
  const u32 vs = WS_V + ((16 * (q >> 1) + (q & 1)) * 448 + 2 * kcol) * 128;
  st128<0>(u32x4{pk(ar[0][0], ar[0][1]), pk(ar[0][2], ar[0][3]), pk(ar[1][0], ar[1][1]), pk(ar[1][2], ar[1][3])}, vv, rws, vs);
  st128<128>(u32x4{pk(ai[0][0], ai[0][1]), pk(ai[0][2], ai[0][3]), pk(ai[1][0], ai[1][1]), pk(ai[1][2], ai[1][3])}, vv, rws, vs);
}
#define VFS 32
DI void v_publish(u32 syncw, GAS char* ws, int lane, int k, u32 tlo, u32 thi) {
  wait_vm<0>();
  if (lds_arrive(syncw) == 3 && lane == 0) set_flag(ws, WS_FLAG_V + (((k >> 2) & 3) * 56 + (k >> 4) * 4 + (k & 3)) * VFS, tlo, thi);
}

struct Fin { u32x4 xr; float r2; u32 ov, os, pa, qa; };
DI void fin_load(const Ctx& c, Fin& f, int lane, u32x4 sx) {
  const int mi2 = c.tau >> 1;
  f.os = (1024 * (mi2 >> 3) + 512 * (c.tau & 1) + 128 * ((mi2 >> 1) & 3) + 16 * (mi2 & 1) + 32 * (c.w >> 1) + 8 * (c.w & 1)) * 896 + c.nt * 128;
  f.ov = (lane >> 3) * 896 + (lane & 7) * 16;
  ld128<0>(f.xr, f.ov, sx, f.os);
  ld32(f.r2, (lane >> 3) * 4, c.sws, WS_R2 + c.tau * 256 + c.w * 32);
  const int d = c.w >> 1, r16 = 8 * (c.w & 1) + (lane >> 3);
  f.pa = pv(d * SLOT + r16 * 256 + ((2 * (lane & 7)) ^ r16) * 16);
  f.qa = pv(4 * SLOT + d * 4096 + r16 * 256 + ((2 * (lane & 7)) ^ r16 ^ (2 * d + (c.w & 1))) * 16);
}
DI void fin_run(const Ctx& c, Fin& f, u32x4 so) {
  const u32 pa = f.pa, pb = pv(pa ^ 16), qa = f.qa, qb = pv(qa ^ 16);
  pin(f.xr); pin(f.r2);
  f32x4 x0 = {bflo(f.xr[0]), bfhi(f.xr[0]), bflo(f.xr[1]), bfhi(f.xr[1])}, x1 = {bflo(f.xr[2]), bfhi(f.xr[2]), bflo(f.xr[3]), bfhi(f.xr[3])};
  pin(x0); pin(x1);
  wait_lgkm0();
  barrier();
  f32x4 p[2][5];
#pragma unroll
  for (int i = 0; i < 4; ++i) { p[0][i] = *(LAS f32x4*)(c.L + pa + i * 4096); p[1][i] = *(LAS f32x4*)(c.L + pb + i * 4096); }
  p[0][4] = *(LAS f32x4*)(c.L + qa); p[1][4] = *(LAS f32x4*)(c.L + qb);
  SB();
  const f32x4 o0 = ((p[0][0] + p[0][1]) + (p[0][2] + p[0][3])) * f.r2 + x0 + p[0][4], o1 = ((p[1][0] + p[1][1]) + (p[1][2] + p[1][3])) * f.r2 + x1 + p[1][4];
  st128_plain(u32x4{pk(o0[0], o0[1]), pk(o0[2], o0[3]), pk(o1[0], o1[1]), pk(o1[2], o1[3])}, f.ov, so, f.os);
}

#define NB 12
#define PAD 16
#define PAD0 0
#define PAD1 27
#define SL7 0
#define CP7 1
#define CP55 0
#define CA_G 27
#define HA_J 10
#define PS_G 51
#define HWIN 3
#define FB 2
constexpr int CB_G = 48, ZE_G = 29;
struct CState {
  u32x4 xf[14][2], wf[2][4], ff[2], wd[14][4];
  u32 ad[2][4];
  f32x4 acc[2][4][2], az[2][2];
  u32 ax[2], aw[2][2], aw4[2][2], nm[2], sh, acs, anm, aht, af;
  float ri[2];
  u32x2 cs[2], bx[4];
  u32x2 fl, ep;
  u32 flo;
};
DI u32x4 c_h(const CState& s, int p, int m) {
  const f32x4 h0 = s.acc[p][0][m] * s.acc[p][2][m], h1 = s.acc[p][1][m] * s.acc[p][3][m];
  return u32x4{pk(h0[0], h0[1]), pk(h0[2], h0[3]), pk(h1[0], h1[1]), pk(h1[2], h1[3])};
}
template <int J> DI void c_barrier(Ctx& c) {
  if constexpr (J == HA_J) wait_vm<0>();
  if constexpr (J >= 8 && J <= 10) asm volatile("s_waitcnt lgkmcnt(8)" ::: "memory");
  barrier();
  if constexpr (J == 0) CK2(0);
  if constexpr (J == 4) CK2(1);
  if constexpr (J == 7) CK2(2);
  if constexpr (J == 10) CK2(3);
  if constexpr (J == 0 || J == 7 || J == 9 || J == 12) STAMP(16 + J);
}
template <int G> DI void c_group(Ctx& c, CState& s) {
  constexpr int N = G + 1, J = N >> 2, p = G & 1, kk = G < 0 ? 0 : (G >> 1) % 14;
  if constexpr ((N & 3) == 0 && J <= NB) c_barrier<J>(c);
  if constexpr (G == 27 && CP7) __builtin_amdgcn_s_setprio(3);
  if constexpr (G == 29 && CP7) __builtin_amdgcn_s_setprio(CPRIO);
  if constexpr (G == 55 && CP55) __builtin_amdgcn_s_setprio(3);
  constexpr bool CORR = G == CA_G || G == CB_G || G == CB_G + 1;
  constexpr bool EPI = G == 28 || G == 55;
  constexpr u32 o = (w_slot(J) & 3) * SLOT + ((N >> 1) & 1) * 8192;
  u32x4 ho[2];
  u32x2 half, half2;
  float zt[4];
  auto epi = [&](int pp, int i) {
    const int m = i & 1, v = (i >> 1) & 1;
    float h[4];
#pragma unroll
    for (int j = 0; j < 4; ++j) h[j] = mul1(s.acc[pp][v][m][j], s.acc[pp][2 + v][m][j]);
    ho[m][2 * v] = pv(pk(h[0], h[1])); ho[m][2 * v + 1] = pv(pk(h[2], h[3]));
    if (G < 55 && v) { if (m) st128<1024>(ho[m], c.l16, c.sws, s.sh + pp * 4096); else st128<0>(ho[m], c.l16, c.sws, s.sh + pp * 4096); }
  };
  auto csr = [&](int pp, int i) { s.cs[i] = *(LAS u32x2*)(c.L + s.acs + (G > 27 ? L_CSB - L_CS : 0) + pp * 256 + i * 128); };
  SB();
  if constexpr (p && G < 28) {
#pragma unroll
    for (int i = 0; i < 4; ++i) {
      if constexpr (G >= 0) { if (kk) mma(s.az[i >> 1][i & 1], s.ff[i >> 1], s.xf[kk][i & 1]); else mma0(s.az[i >> 1][i & 1], s.ff[i >> 1], s.xf[kk][i & 1]); }
      SB();
      if constexpr (N < 28) {
        if (i < 2) s.xf[N >> 1][i] = lds128(c.L, s.ax[(N >> 1) & 1] + (J % 4) * SLOT + i * 2048);
        else s.ff[i & 1] = lds128(c.L, s.af + (N + (i & 1)) * 1024);
      } else {
        if (i < 2) { s.nm[i] = *(LAS u32*)(c.L + s.anm + 64 * i); csr(0, i); }
      }
      SB();
    }
  }
#pragma unroll
  for (int i = 0; i < 8; ++i) {
    if constexpr (G >= 0) { if (kk) mma(s.acc[p][i >> 1][i & 1], s.wf[G & 1][i >> 1], s.xf[kk][i & 1]); else mma0(s.acc[p][i >> 1][i & 1], s.wf[G & 1][i >> 1], s.xf[kk][i & 1]); }
    SB();
    if constexpr (N < 56) {
      const u32x2 v = lds_tr1(c.L, (J == 9 ? s.aw4 : s.aw)[N & 1][(i >> 1) & 1] + o + (i >> 2) * 1024 + (i & 1) * 512);
      if (i & 1) s.wf[N & 1][i >> 1] = cat(half, v); else half = v;
    }
    if constexpr (CORR && G > 27) { if (i == 1 || i == 2) csr(p, i & 1); }
    if constexpr (G == CA_G) {
      if (i == 3 || i == 4) { s.bx[i & 1] = u32x2{s.nm[i & 1], 0u}; pin(s.bx[i & 1]); }
      if (i == 5 || i == 6) { s.bx[2 + (i & 1)] = u32x2{0u, s.bx[i & 1][0]}; pin(s.bx[2 + (i & 1)]); }
    }
    if constexpr (G == 34) { if (i & 1) s.aw4[i >> 2][(i >> 1) & 1] = pv(s.aw[i >> 2][(i >> 1) & 1] + 4 * SLOT); }
    if constexpr (G == 30) {
      const u32 l = c.l16 >> 4, g = l >> 4, qq = (l >> 2) & 3, pq = l & 3;
      if (i == 1) s.ad[1][0] = pv(5 * SLOT + c.lb + (4 * g + qq) * 128 + (((pq >> 1) ^ (2 * ((qq >> 1) | ((g & 1) << 1)))) * 16) + (pq & 1) * 8);
      if (i > 2 && i < 6) s.ad[1][i - 2] = pv(s.ad[1][0] ^ (32 * (i - 2)));
    }
    if constexpr (G == 32 || G == 33 || G == 36 || G == 37) {
      constexpr int y = G < 36 ? G - 32 : G - 34;
      const u32x2 v = lds_tr1(c.L, s.ad[1][i >> 1] + (wd_slot(y) - 5) * SLOT + (i & 1) * 2048);
      if (i & 1) s.wd[y][i >> 1] = cat(half2, v); else half2 = v;
    }
    if constexpr (G == 41) { if (i == 0) s.ep = *(LAS u32x2*)(c.L + Y_TAB); }
    if constexpr (G == 44) { if (i == 0) { c.tlo = __builtin_amdgcn_readfirstlane(s.ep[0]); c.thi = __builtin_amdgcn_readfirstlane(s.ep[1]); } }
    if constexpr (EPI) { if (EPI0 == 4 ? i >= 4 : (i & 1)) epi(!p, EPI0 == 4 ? i : i >> 1); }
    if constexpr (G == 26) { if (i == 2) s.ax[0] = pv(L_RI + 128 * (c.w & 3) + ((c.l16 >> 2) & 60)); if (i == 4) s.af = pv(L_XCH + (c.w & 3) * 4 + ((c.l16 & 0x300) << 2) + (c.l16 & 0xf0)); }
    if constexpr (G == ZE_G - 1) { if (i < 2) s.ri[i] = *(LAS float*)(c.L + s.ax[0] + 64 * i); }
    if constexpr (G == ZE_G) {
      if (i == 5 && c.ni == 0) { s.az[0][0][0] += 448.f * bfhi(s.bx[0][0]); s.az[0][1][0] += 448.f * bfhi(s.bx[1][0]); }
      const int j = (7 - i) & 6;
      if (!(i & 1)) {
#pragma unroll
        for (int e = 0; e < 4; ++e) zt[e] = mul1(s.az[j >> 2][e & 1][(j & 3) + (e >> 1)], s.ri[e & 1]);
      } else {
        *(LAS u32*)(c.L + s.af + (j >> 2) * 4096 + (j & 3) * 256) = pk(zt[0], zt[1]);
        *(LAS u32*)(c.L + s.af + (j >> 2) * 4096 + (j & 3) * 256 + 256) = pk(zt[2], zt[3]);
      }
    }
    SB();
  }
  if constexpr (PAD > 0 && G >= PAD0 && G < PAD1) asm volatile("s_nop %0" :: "n"(PAD - 1));
  if constexpr (N == 55) lds_post(Y_FREE);
  if constexpr (CORR) {
    constexpr int pp = G == CA_G ? 0 : p;
#pragma unroll
    for (int i = 0; i < 8; ++i) {
      mmak16(s.acc[pp][i >> 1][i & 1], s.cs[i >> 2], s.bx[i & 3]);
      if constexpr (G == CA_G) { SB(); if (i == 3 || i == 7) csr(1, i >> 2); SB(); }
    }
    if constexpr (G == CA_G) {
      asm volatile("s_nop 4");
#pragma unroll
      for (int i = 0; i < 8; ++i) { SB(); if (!(i & 1)) epi(0, i >> 1); SB(); mmak16(s.acc[1][i >> 1][i & 1], s.cs[i >> 2], s.bx[i & 3]); }
    }
  }
  if constexpr (G == 55) { st128<0>(ho[0], c.l16, c.sws, s.sh + 8192); st128<1024>(ho[1], c.l16, c.sws, s.sh + 8192); }
  if constexpr (G == PS_G) { s.flo = WS_FLAG_A + (c.tau >> 1) * 2048 + (c.w & 2) * 64 + (c.l16 < 112 ? c.l16 * 16 : 0); ld64(s.fl, s.flo, c.sws); }
  SB();
  if constexpr (G < 55) c_group<G + 1>(c, s);
}

#define VP_Y 6
#define BS_Y 3
#define BC_Y 5
#define HW0 2
#define HW1 3
#define RF0 1
#define RF3 4
#define BURST 2
constexpr int NPRE = 0;
static_assert(NB == 12, "");
constexpr int g2_rf(int Y) { return Y >= RF0 && Y < RF0 + 3 ? 40 + Y - RF0 : Y == RF3 ? 43 : -1; }
constexpr int g2_nr(int Y) { return g2_rf(Y) > 0 ? 4 : 0; }
constexpr int g2_hi(int Y) {
  int n = HW1;
  for (int q = 0; q <= Y; ++q) {
    if (q == BC_Y) n += BURST;
    if (q == Y) break;
    const int cap = q < BC_Y ? 7 : q + 7 < 14 ? q + 7 : 14;
    n += g2_nr(q) ? 1 : 2; n = n > cap ? cap : n;
  }
  return n;
}
constexpr int g2_in(int Y) { return g2_hi(Y + 1) - g2_hi(Y) - (Y + 1 == BC_Y ? BURST : 0); }
constexpr int g2_young(int Y, int H, int R, int Smp) {
  int n = 99;
  for (int h = 0; h < HW1; ++h) n = h == H ? 0 : n + 4;
  n = R == -1 ? 0 : n + NPRE;
  for (int q = 0; q <= Y; ++q) {
    if (q == BS_Y) n = Smp ? 0 : n + 1;
    if (q == BC_Y) { if (Smp) break; for (int h = 7; h < 7 + BURST; ++h) n = h == H ? 0 : n + 4; }
    if (q == Y) break;
    for (int i = 0; i < 8; ++i) {
      const bool two = g2_in(q) == 2;
      const int h = two ? g2_hi(q) + (i >> 2) : (g2_in(q) == 1 && !(i & 1)) ? g2_hi(q) : -9;
      if (h >= 0) n = h == H ? 0 : n + 1;
      else if (g2_nr(q) && (i & 1)) n = R == q ? 0 : n + 1;
    }
  }
  return n > 63 ? 63 : n;
}
constexpr int g2_wait(int Y) {
  const int a = g2_young(Y, Y, -9, 0), b = Y >= 9 ? g2_young(Y, -9, Y == 11 ? RF3 : RF0 + Y - 9, 0) : 63;
  return a < b ? a : b;
}
static_assert(RF3 >= 4 && RF3 >= RF0 + 3 && BS_Y < BC_Y && BC_Y <= 7 && BURST <= BC_Y && g2_hi(BC_Y) == 7 + BURST && g2_hi(12) == 14, "");
template <int H> DI u32 h_off(const Ctx& c, u32 hs) { u32 v = hs + rot7<H % 7>(c.rot) * 32768 + (H / 7) * 8192; pins(v); return v; }
template <int I, int END> DI void h_req(const Ctx& c, u32x4 (&hf)[7][4], u32 hs) {
  if constexpr (I < END) {
    ld128x4<(I / 7) * 8192>(hf[I % 7][0], hf[I % 7][1], hf[I % 7][2], hf[I % 7][3], c.l16, c.sws, hs + rot7<I % 7>(c.rot) * 32768);
    h_req<I + 1, END>(c, hf, hs);
  }
}
template <int Y> DI void g2_step(const Ctx& c, const u32 (&ad)[2][4], u32 hs, u32 flo, u32x2& fl, u32x4 (&hf)[7][4], f32x4 (&acc)[4][4], u32x4 (&wd)[14][4]) {
  constexpr int hb = Y % 7, H = g2_hi(Y), NI = g2_in(Y);
  if constexpr (Y == BS_Y) ld64(fl, flo, c.sws);
  if constexpr (Y == BC_Y) {
    STAMP(4);
    wait_vm<g2_young(Y, -9, -9, 1)>();
    pin(fl);
    CNT(27, poll_flags(fl, flo, c.sws, c.tlo, c.thi));
    STAMP(5);
    h_req<7, 7 + BURST>(c, hf, hs);
  }
  wait_vm<g2_wait(Y)>();
  pin(hf[hb][0]); pin(hf[hb][1]); pin(hf[hb][2]); pin(hf[hb][3]);
  if constexpr (Y == 3) lds_wait(Y_DL, 4);
  if constexpr (Y == 6) lds_wait(Y_VD, 4);
  if constexpr (Y == RF0) lds_wait(Y_FREE, 4);
  constexpr int RF = g2_rf(Y);
  if constexpr (RF == 43 && Y == 4) wait_lgkm0();
  u32 rso = 0, h0 = 0, h1 = 0;
  if constexpr (NI > 0) h0 = h_off<H>(c, hs);
  if constexpr (NI > 1) h1 = h_off<H + 1>(c, hs);
  if constexpr (RF > 0) rso = stage_open<RF>(c);
  SB();
  u32x2 half;
#pragma unroll
  for (int i = 0; i < 16; ++i) {
    if (Y) mma(acc[i & 3][i >> 2], wd[Y][i & 3], hf[hb][i >> 2]); else mma0(acc[i & 3][i >> 2], wd[Y][i & 3], hf[hb][i >> 2]);
    SB();
    if constexpr (Y >= 3) {
      if (i < 8 || Y == 11) {
        const int y = Y + 1 + (i >> 3), sl = wd_slot(y);
        const u32x2 v = lds_tr1(c.L, (sl >= 5 ? ad[1][(i >> 1) & 3] + (sl - 5) * SLOT : sl == 4 ? ad[1][(i >> 1) & 3] - SLOT : ad[0][(i >> 1) & 3] + sl * SLOT) + (i & 1) * 2048);
        if (i & 1) wd[y][(i >> 1) & 3] = cat(half, v); else half = v;
      }
    }
    if constexpr (NI == 1) {
      if (i == 1) ld128<0>(hf[H % 7][0], c.l16, c.sws, h0);
      if (i == 5) ld128<1024>(hf[H % 7][1], c.l16, c.sws, h0);
      if (i == 9) ld128<2048>(hf[H % 7][2], c.l16, c.sws, h0);
      if (i == 13) ld128<3072>(hf[H % 7][3], c.l16, c.sws, h0);
    }
    if constexpr (NI == 2) {
      if (i == 1) ld128<0>(hf[H % 7][0], c.l16, c.sws, h0);
      if (i == 3) ld128<1024>(hf[H % 7][1], c.l16, c.sws, h0);
      if (i == 5) ld128<2048>(hf[H % 7][2], c.l16, c.sws, h0);
      if (i == 7) ld128<3072>(hf[H % 7][3], c.l16, c.sws, h0);
      if (i == 9) ld128<0>(hf[(H + 1) % 7][0], c.l16, c.sws, h1);
      if (i == 11) ld128<1024>(hf[(H + 1) % 7][1], c.l16, c.sws, h1);
      if (i == 13) ld128<2048>(hf[(H + 1) % 7][2], c.l16, c.sws, h1);
      if (i == 15) ld128<3072>(hf[(H + 1) % 7][3], c.l16, c.sws, h1);
    }
    if constexpr (RF > 0) {
      if (i == 3) stage_one<RF, 0>(c, rso);
      if (i == 7) stage_one<RF, 1>(c, rso);
      if (i == 11) stage_one<RF, 2>(c, rso);
      if (i == 15) stage_one<RF, 3>(c, rso);
    }
    SB();
  }
  if constexpr (Y == 3) CK2(5);
  if constexpr (Y == VP_Y && VP_Y != 6) lds_post(Y_VP);
  if constexpr (Y == 6) lds_post(Y_V1);
  if constexpr (Y == 7) lds_post(Y_VS);
  if constexpr (Y < 11) g2_step<Y + 1>(c, ad, hs, flo, fl, hf, acc, wd);
}

DI void compute_waves(Ctx& c, const void* x, const void* wd_, void* out, void* ws) {
  int lane = c.lane, r16 = lane & 15, g = lane >> 4, qq = (lane >> 2) & 3, pq = lane & 3;
  const int t = c.w & 3;
#pragma unroll
  for (int pp = 0; pp < 2; ++pp) {
    const int k = 16 * c.ni + 8 * pp + (r16 >> 1);
    const bool nyq = (k == 0) && (r16 & 1);
    const float phi = nyq ? 0.5f : (float)k * (1.f / 448.f), o = ((r16 & 1) && !nyq) ? 0.25f : 0.f;
    const float c2 = 2.f * cosr(128.f * phi);
    const float p0 = __builtin_amdgcn_fractf((float)(64 * (int)c.rot1 + 32 * t + 8 * g) * phi) + o;
    float va[8], vb[8];
#pragma unroll
    for (int j = 0; j < 8; ++j) { va[j] = cosr((float)j * phi + p0); vb[j] = cosr((float)(j + 128) * phi + p0); }
    const u32 fb = L_F + (2 * t + pp) * 1024 + lane * 16;
    *(LAS u32x4*)(c.L + fb) = u32x4{pk(va[0], va[1]), pk(va[2], va[3]), pk(va[4], va[5]), pk(va[6], va[7])};
    *(LAS u32x4*)(c.L + fb + 8192) = u32x4{pk(vb[0], vb[1]), pk(vb[2], vb[3]), pk(vb[4], vb[5]), pk(vb[6], vb[7])};
#pragma unroll
    for (int j = 0; j < 8; ++j) va[j] = c2 * vb[j] - va[j];
    *(LAS u32x4*)(c.L + fb + 16384) = u32x4{pk(va[0], va[1]), pk(va[2], va[3]), pk(va[4], va[5]), pk(va[6], va[7])};
    if (t < 2) {
#pragma unroll
      for (int j = 0; j < 8; ++j) vb[j] = c2 * va[j] - vb[j];
      *(LAS u32x4*)(c.L + fb + 24576) = u32x4{pk(vb[0], vb[1]), pk(vb[2], vb[3]), pk(vb[4], vb[5]), pk(vb[6], vb[7])};
    }
  }
  if (t == 0) *(LAS u32*)(c.L + L_SYNC + lane * 4) = 0u;
  CState s;
  c.sws = mk_srd(ws, WS_END); c.sd = mk_srd(wd_, W_BYTES);
  u32 sg2 = 2 * ((qq >> 1) | ((g & 1) << 1));
  c.l16 = lane * 16;
  s.ax[0] = 4 * SLOT + (32 * t + r16) * 128 + ((g ^ (r16 >> 1)) * 16);
  s.ax[1] = s.ax[0] ^ 64;
  s.aw[0][0] = g * 2048 + qq * 128 + (((pq >> 1) ^ sg2) * 16) + (pq & 1) * 8;
  s.aw[0][1] = s.aw[0][0] ^ 32; s.aw[1][0] = s.aw[0][0] ^ 64; s.aw[1][1] = s.aw[0][0] ^ 96;
  s.sh = WS_H + (((2 * c.mi + (t >> 1)) * 56 + 4 * c.ni) * 4 + 2 * (t & 1)) * 1024;
  s.acs = pv(L_CS + r16 * 8); s.anm = pv(lane < 16 ? L_NM + 128 * t + r16 * 4 : L_ZERO); s.af = pv(L_F + c.l16); s.aht = pv(L_HT + c.lb + c.l16);
  pin(c.l16); pin(s.ax[0]); pin(s.ax[1]); pin(s.aw[0][0]); pin(s.aw[0][1]); pin(s.aw[1][0]); pin(s.aw[1][1]); pins(s.sh);
  wait_lgkm0();
  __builtin_amdgcn_s_setprio(CPRIO);
  STAMP(1);
  c_group<-1>(c, s);
  mma_pad();
  SB();
  { const u32x4 o0 = c_h(s, 1, 0), o1 = c_h(s, 1, 1); st128<0>(o0, c.l16, c.sws, s.sh + 12288); st128<1024>(o1, c.l16, c.sws, s.sh + 12288); }
  SB();
  if constexpr (CP55) __builtin_amdgcn_s_setprio(CPRIO);
  STAMP(2);
  CK2(4);
  const u32 hs = WS_H + (c.tau * 56 + 4 * (t >> 1) + (t & 1)) * 4096;
  u32x4 hf[7][4];
  f32x4 acc[4][4];
  wait_vm<4>();
  pin(s.fl);
  CNT(26, poll_flags(s.fl, s.flo, c.sws, c.tlo, c.thi));
  STAMP(3);
  h_req<0, HW0>(c, hf, hs);
  lane = pv(c.l16) >> 4; r16 = lane & 15; g = lane >> 4; qq = (lane >> 2) & 3; pq = lane & 3; sg2 = 2 * ((qq >> 1) | ((g & 1) << 1));
#pragma unroll
  for (int v = 0; v < 4; ++v) { s.ad[0][v] = pv(s.ad[1][v] - 5 * SLOT); c.vwd[v] = pv(wd_src(lane, v)); }
  wait_vm<4 * HW0>();
  STAMP(9);
  if (lds_arrive(Y_HB) == 3 && lane == 0) set_flag(c.ws, WS_FLAG_B + (c.mi * 16 + c.ni) * 128, c.tlo, c.thi);
  h_req<HW0, HW1>(c, hf, hs);
  STAMP(10);
  g2_step<0>(c, s.ad, hs, s.flo + (WS_FLAG_B - WS_FLAG_A), s.fl, hf, acc, s.wd);
  u32 pa[4];
#pragma unroll
  for (int v = 0; v < 4; ++v) pa[v] = pv(t * 4096 + r16 * 256 + (((4 * v + g) ^ r16) * 16));
  Fin fin;
  fin_load(c, fin, lane, mk_srd(x, X_BYTES));
  wait_vm<2>();
#pragma unroll
  for (int i = 0; i < 8; ++i) pin(hf[(12 + (i >> 2)) % 7][i & 3]);
  wait_lgkm0();
  SB();
#pragma unroll
  for (int i = 0; i < 40; ++i) {
    const int d = i >> 3, y = 12 + ((i >> 2) & 1);
    if (i < 32) mma(acc[i & 3][d], s.wd[y][i & 3], hf[y % 7][d]);
    if (i == 32) { CK2(6); STAMP(6); mma_pad(); }
    SB();
    if (i >= 12 && (i & 4)) {
      const int e = d - 1, v = i & 3;
      *(LAS f32x4*)(c.L + pa[v] + e * SLOT) = acc[v][e];
    }
    SB();
  }
  STAMP(7);
  wait_vm<0>();
  fin_run(c, fin, mk_srd(out, X_BYTES));
  STAMP(11);
}

#define GA_I 5
#define GB_I 10
constexpr int NST = 2;
#define GEN0 7
#define EARLY8 1
constexpr int s_piece(int i, int h) {
  if (i <= 4) return h == 0 ? i + 2 : h == 1 ? 12 + i : -1;
  if (i <= 6) return h == 0 ? 12 + i : h == 1 ? 25 + i : -1;
  if (i == 7) return h < 2 ? 19 + h : h < 4 ? 30 + h : -1;
  if (i <= 10) return h == 0 ? 13 + i : h == 1 ? (i == 8 ? 104 : 25 + i) : -1;
  if (i == 11) return h < 2 ? 36 + h : -1;
  return -1;
}
constexpr int s_wait(int i, int pos, int id) {
  int n = id == 10 ? 9 : id == 11 ? 1 : 64;
  for (int q = 0; q <= i; ++q) {
    const int e = q == GA_I ? 100 : q == GB_I ? 101 : q == NB ? 103 : -1;
    if (e > 0) n = e == id ? 0 : n + 1;
    for (int k = 0; k < (q < i ? 5 : pos); ++k) if (s_piece(q, k) >= 0) n = s_piece(q, k) == id ? 0 : n + (s_piece(q, k) == 104 ? 2 : 4);
    if (q >= T0 && q < T0 + 5 && (q < i || pos == 5)) n += 1;
  }
  return n > 63 ? 63 : n;
}
struct GState { u32x4 gf[14]; float va[8], vb[8], c2, f; };
template <int Q> DI void g_gen(GState& s, int n0, int lane) {
  constexpr float sc = 2.f / (2048.f * 448.f);
  const int r = lane & 15, g = lane >> 4;
  if constexpr (Q < 2) {
    if constexpr (Q == 0) { s.f = pf((float)(n0 + r) * (1.f / 448.f)); s.c2 = pf(2.f * cosr(16.f * s.f)); }
    const float p0 = (float)(4 * g + 16 * Q) * s.f;
#pragma unroll
    for (int j = 0; j < 8; ++j) (Q ? s.vb : s.va)[j] = pf(sc * cosr((float)(j >> 1) * s.f + p0 + ((j & 1) ? 0.25f : 0.f)));
  } else if constexpr (Q < 9) {
    constexpr int kk = 2 * (Q - 2);
#pragma unroll
    for (int j = 0; j < 4; ++j) { s.gf[kk][j] = pv(pk(s.va[2 * j], s.va[2 * j + 1])); s.gf[kk + 1][j] = pv(pk(s.vb[2 * j], s.vb[2 * j + 1])); }
    if constexpr (kk == 0) { if (g == 0) s.gf[0][0] = pk(0.5f * sc, (r & 1) ? -0.5f * sc : 0.5f * sc); pin(s.gf[0]); }
    if constexpr (kk < 12) {
#pragma unroll
      for (int j = 0; j < 8; ++j) s.va[j] = pf(s.c2 * s.vb[j] - s.va[j]);
#pragma unroll
      for (int j = 0; j < 8; ++j) s.vb[j] = pf(s.c2 * s.va[j] - s.vb[j]);
    }
  }
}
template <int Q> DI void g_all(GState& s, int n0, int lane) { g_gen<Q>(s, n0, lane); if constexpr (Q < 8) g_all<Q + 1>(s, n0, lane); }
struct SState {
  GState gs;
  u32x4 gr;
  u32 vga, vgb, ata, aht, sh, sz, flo, vt;
  u32x4 st, st2;
  u32x2 ep, fl2;
  FftC fc; FftL fl; FftIn fi;
};
template <int K> DI void s_fill(const Ctx& c, SState& s) {
  SB();
  __builtin_amdgcn_s_setprio(GPRIO);
  if constexpr (K == 2 && !GLATE) fft_lane(s.fl, c.w, c.lane);
  if constexpr (FLATE < 16) {
    if constexpr (K == 0 && FLATE) fft_lane(s.fl, c.w, c.lane);
    if constexpr (K < FLATE) fft_gen<K>(s.fc, s.fl);
  } else if constexpr (GLATE) {
    if constexpr (K == 0) fft_lane(s.fl, c.w, c.lane);
    fft_gen<K>(s.fc, s.fl);
    if constexpr (K >= 8) fft_gen<K + 4>(s.fc, s.fl);
  } else {
  g_gen<K>(s.gs, 64 * c.nt + 16 * c.w, c.lane);
  if constexpr (K >= 2 && K < 9) fft_gen<K - 2>(s.fc, s.fl);
  if constexpr (K >= 9) { fft_gen<3 * K - 20>(s.fc, s.fl); fft_gen<3 * K - 19>(s.fc, s.fl); fft_gen<3 * K - 18>(s.fc, s.fl); }
  }
  __builtin_amdgcn_s_setprio(SPRIO);
  SB();
}
template <int I> DI void s_iter(Ctx& c, SState& s) {
  if constexpr (I == HA_J) { if (c.w == 1 && c.lane == 0) set_flag(c.ws, WS_FLAG_A + (c.mi * 16 + c.ni) * 128, c.tlo, c.thi); }
  u32x4 ht[6];
  if constexpr (I == 8) {
#pragma unroll
    for (int i = 0; i < 2; ++i) ht[4 + i] = lds128(c.L, L_XCH + c.w * 2048 + c.l16 + i * 1024);
  }
  if constexpr (I == GA_I) ld128<0>(s.gr, s.vga, c.sws, 0u);
  if constexpr (I == GB_I) ld128<0>(s.gr, s.vgb, c.sws, 0u);
  if constexpr (I == NB) ld64(s.fl2, s.flo, c.sws);
  if constexpr (s_piece(I, 0) >= 0) stage_in<s_piece(I, 0)>(c);
  if constexpr (I < 6) s_fill<2 * I>(c, s);
  if constexpr (I == GA_I + 1 || I == GB_I + 1) {
    constexpr int B = I == GB_I + 1;
    wait_vm<s_wait(I, 1, 100 + B)>();
    pin(s.gr);
    u32 sp = 0;
#pragma unroll 1
    for (; sp < SPIN_CAP; ++sp) {
      if (all_zero((gran_lo(s.gr, c.tlo) & 0xffffu) | gran_hi(s.gr, c.thi))) break;
      ld128<0>(s.gr, B ? s.vgb : s.vga, c.sws, 0u);
      wait_vm<0>();
      pin(s.gr);
    }
    CNT(24 + B, sp);
    if constexpr (B) {
      if (c.lane < 32) *(LAS u32*)(c.L + s.ata + (L_CSB - L_CS)) = __builtin_amdgcn_perm(s.gr[2], s.gr[0], 0x07060302u);
    } else {
      *(LAS u32*)(c.L + s.ata) = __builtin_amdgcn_perm(s.gr[2], s.gr[0], c.lane < 32 ? 0x07060302u : 0x03020302u);
      if (c.lane >= 32) *(LAS float*)(c.L + s.ata + (L_RI - L_NM)) = __builtin_amdgcn_rcpf(bfhi(s.gr[2]));
    }
  }
  if constexpr (I == 7) *(LAS u32x2*)(c.L + Y_TAB) = u32x2{c.tlo, c.thi};
  if constexpr (I == 8) {
    const u32 zo = ((c.l16 & 0x300) << 4) + (c.l16 & 0xf0);
    st128<0>(ht[4], zo, c.sws, s.sz); st128<0>(ht[5], zo, c.sws, s.sz + 16384);
  } else if constexpr (s_piece(I, 1) >= 0) stage_in<s_piece(I, 1)>(c);
  if constexpr (I == 7 && SL7 > 0) __builtin_amdgcn_s_sleep(SL7);
  if constexpr (s_piece(I, 2) >= 0) stage_in<s_piece(I, 2)>(c);
  if constexpr (s_piece(I, 3) >= 0) stage_in<s_piece(I, 3)>(c);
  if constexpr (I >= T0 && I < T0 + 5) {
    constexpr int K = I - T0;
    const u32 rr = (K < 4 ? K : c.w) * 224 + c.kcol, sdw = ((rr >> 6) * 128 + (rr & 63) + (K < 4 ? 0 : 64)) * 896;
    if constexpr (K < 4) touch(s.vt, K < 2 ? s.st : s.st2, c.w < 2 ? K * 1568 * 256 : c.w == 2 ? sdw : (K & 1) * 3600 * 128);
    else touch(c.lane < 7 ? c.l16 * 8 : 0x7ffffff0u, c.sd, sdw);
  }
  if constexpr (I < 6) s_fill<2 * I + 1>(c, s);
  if constexpr (I < NB) {
    constexpr int n1 = s_wait(I, 5, I + 1 == NB ? 23 : I == 8 ? 33 : I == 7 ? 31 : 11 + I), n2 = I + 1 == HA_J ? s_wait(I, 5, 104) : 63;
    if constexpr (I == GA_I + 1 || I == GB_I + 1 || I == 7) wait_lgkm0();
    if constexpr (I == 1) wait_tag<n1>(s.ep, c.tlo, c.thi); else wait_vm<(n1 < n2 ? n1 : n2)>();
    barrier();
    SB();
    s_iter<I + 1>(c, s);
  }
}
template <int K> DI void fft_gen_upto(FftC& k, const FftL& l) { fft_gen<K>(k, l); if constexpr (K + 1 < PGN) fft_gen_upto<K + 1>(k, l); }
template <int KK> DI void v_stage(u32 v, u32x4 sws, u32 sb, u32 lb) {
  glds1_sc1<(KK < 12 ? 5 * SLOT + KK * 4096 : L_XCH + (KK - 12) * 4096), KK * 4096>(v, sws, sb, lb);
  if constexpr (KK < 13) v_stage<KK + 1>(v, sws, sb, lb);
}

DI void service_waves(Ctx& c, const void* wr, const void* wi, const void* wd_, void* out, void* ws) {
  const int lane = c.lane, q = c.w & 3, r = lane & 15, g = lane >> 4;
  __builtin_amdgcn_s_setprio(SPRIO);
  SState s;
  c.sd = mk_srd(wd_, W_BYTES); c.sws = mk_srd(ws, WS_END);
#pragma unroll
  for (int i = 0; i < 4; ++i) c.vwd[i] = wd_src(lane, i);
  c.l16 = lane * 16;
  SB();
  ld64(s.ep, 0u, c.sws);
  s.vgb = WS_CS + ((q >> 1) * 1792 + 128 * c.ni + 32 * (q & 1) + (lane & 31)) * 16;
  s.vga = pv(lane < 32 ? s.vgb : WS_ST + (c.mi * 128 + 32 * q + (lane & 31)) * 16);
  s.vgb = pv(s.vgb + 1024);
  s.ata = pv(lane < 32 ? L_CS + (q & 1) * 256 + (q >> 1) * 128 + r * 8 + g * 4 : L_NM + (32 * q + (lane & 31)) * 4);
  s.aht = pv(L_HT + c.lb + c.l16);
  s.sh = WS_H + (((2 * c.mi + (q >> 1)) * 56 + 4 * c.ni) * 4 + 2 * (q & 1)) * 1024;
  s.sz = WS_Z + (c.ni * 32 + q * 8) * 4096 + c.mi * 256;
  s.flo = pv(lane < 16 ? WS_FLAG_A + (lane * 16 + (c.kcol >> 4)) * 128 : WS_FLAG_W + ((lane & 7) * 4 + q) * 128);
  pins(s.sh); pins(s.sz);
  s.vt = pv(q == 3 ? (lane < 17 ? (c.kcol * 16 + lane) * 128 : 0x7ffffff0u) : lane >= 7 ? 0x7ffffff0u : q == 2 ? lane * 128 : (c.kcol * 7 + lane) * 256 + 128 + 1024 * q);
  s.st = q == 0 ? c.sg : q == 1 ? c.su : q == 2 ? c.sd : mk_srd(wr, (TMASK & 4) ? FILT_BYTES : 0);
  s.st2 = q < 3 ? s.st : mk_srd(wi, (TMASK & 4) ? FILT_BYTES : 0);
  pins(s.st); pins(s.st2);
  STAMP(31);
  wait_vm<9>();
  STAMP(1);
  barrier();
  s_iter<0>(c, s);
  STAMP(2);
  const int tau = c.tau, mi2 = tau >> 1;
  if constexpr (EARLY8) stage_in<38>(c);
  if constexpr (FLATE < 16) { SB(); __builtin_amdgcn_s_setprio(GPRIO2); if constexpr (FLATE == 0) fft_lane(s.fl, q, lane); fft_gen_rng<FLATE, FMID>(s.fc, s.fl); __builtin_amdgcn_s_setprio(SPRIO); SB(); }
  wait_vm<(EARLY8 ? 4 : 0)>();
  pin(s.fl2);
  CNT(26, poll_flags(s.fl2, s.flo, c.sws, c.tlo, c.thi));
  STAMP(3);
  fft_load(s.fi, c.sws, q, lane, c.kcol, c.kcol);
  if constexpr (!EARLY8) stage_in<38>(c);
  stage_in<39>(c);
  if constexpr (FLATE < 16) { SB(); __builtin_amdgcn_s_setprio(GPRIO2); fft_gen_rng<FMID, 16>(s.fc, s.fl); SB(); }
  if constexpr (GLATE == 2) { SB(); __builtin_amdgcn_s_setprio(GPRIO2); g_all<0>(s.gs, 64 * c.nt + 16 * q, lane); SB(); }
  wait_vm<(EARLY8 ? 4 : 8)>();
  lds_post(Y_DL);
  fft_pin(s.fi);
  __builtin_amdgcn_s_setprio(FPRIO);
  STAMP(13);
  if (c.kcol != 0) fft_run<0>(s.fc, s.fi, c.L, L_XCH, Y_XCH, c.sws, q, 0, lane, c.kcol);
  STAMP(4);
  const u32 vsrc = pv((lane >> 3) * 128 + (((lane & 7) ^ (2 * (g & 1)) ^ (4 * (q & 1))) * 16));
  const int qq = (lane >> 2) & 3, pq = lane & 3;
  u32 av[2][2], aw[2];
#pragma unroll
  for (int m = 0; m < 2; ++m) {
    av[0][m] = pv(5 * SLOT + (8 * g + qq) * 128 + (((4 * (q & 1) + 2 * m + (pq >> 1)) ^ (2 * ((qq >> 1) | ((g & 1) << 1)))) * 16) + (pq & 1) * 8);
    av[1][m] = pv(av[0][m] + (L_XCH - 5 * SLOT));
    const int d = r & 3, r16 = 8 * ((r >> 2) & 1) + 4 * (q & 1) + 2 * m + (r >> 3);
    aw[m] = pv(4 * SLOT + d * 4096 + r16 * 256 + ((4 * q + g) ^ r16 ^ (2 * d + (r16 >> 3))) * 16);
  }
  const u32 vfl = pv(WS_FLAG_V + (q * 56 + (lane < 56 ? lane : 0)) * VFS);
  GState g2;
  g_gen<0>(g2, 64 * c.nt + 16 * (q ^ 1), lane); g_gen<1>(g2, 0, lane); g_gen<2>(g2, 0, lane);
  SB();
  if (c.kcol != 0) v_publish(Y_VD, c.ws, lane, c.kcol, c.tlo, c.thi); else { wait_vm<0>(); lds_post(Y_VD); }
  STAMP(5);
  __builtin_amdgcn_s_setprio(G2PRIO);
  g_gen<3>(g2, 0, lane); g_gen<4>(g2, 0, lane); g_gen<5>(g2, 0, lane); g_gen<6>(g2, 0, lane); g_gen<7>(g2, 0, lane); g_gen<8>(g2, 0, lane);
  if constexpr (GLATE == 1) g_all<0>(s.gs, 64 * c.nt + 16 * q, lane);
  SB();
  __builtin_amdgcn_s_setprio(TPRIO);
  lds_wait<LSL>(VP_Y == 6 ? Y_V1 : Y_VP, 4);
  STAMP(6);
  CNT(27, poll_flags<VSLEEP>(u32x2{~c.tlo, 0u}, vfl, c.sws, c.tlo, c.thi));
  STAMP(7);
  if constexpr (VP_Y < 6) lds_wait<1>(Y_V1, 4);
  v_stage<0>(vsrc, c.sws, VBASE + (tau * 448 + q * 8) * 128, q * 1024);
  Fin fin;
  fin_load(c, fin, lane, c.sx);
  lds_wait(Y_VS, 4);
  wait_vm<16 - VC0>();
  lds_sync(Y_VL, 4);
  STAMP(8);
  u32x4 vf[3][2];
  u32 pkv = 0;
  f32x4 acc[2][2];
  u32x2 half;
#pragma unroll
  for (int kk = -2; kk < 14; ++kk) {
    if (kk + 2 + VPD == VC0) { wait_vm<2>(); lds_post(Y_VL + 4); }
    if (kk + 3 == VC0) pkv = lds_peek(c.L, Y_VL + 4);
    if (kk + 2 == VC0) lds_check(pkv, Y_VL + 4, 4);
#pragma unroll
    for (int i = 0; i < 4; ++i) {
      if (kk > 0) mma(acc[i & 1][i >> 1], (i & 1) ? g2.gf[kk] : s.gs.gf[kk], vf[kk % 3][i >> 1]);
      if (kk == 0) mma0(acc[i & 1][i >> 1], (i & 1) ? g2.gf[0] : s.gs.gf[0], vf[0][i >> 1]);
      SB();
      const int n = kk + 2;
      if (n < 14) {
        const u32x2 v = lds_tr1(c.L, av[n >= 12][i >> 1] + (n % 12) * 4096 + (i & 1) * 512);
        if (i & 1) vf[n % 3][i >> 1] = cat(half, v); else half = v;
      }
      SB();
    }
  }
  STAMP(9);
  mma_pad();
  SB();
#pragma unroll
  for (int i = 0; i < 4; ++i) *(LAS f32x4*)(c.L + (aw[i >> 1] ^ (64 * (i & 1)))) = acc[i & 1][i >> 1];
  wait_vm<0>();
  fin_run(c, fin, mk_srd(out, X_BYTES));
  if (c.kcol == 1 && q == 0 && lane == 0) set_flag(c.ws, 0, c.tlo, c.thi);
  STAMP(11);
}

template <int I, int N, u32 LS, u32 SS> DI void glds_n(u32 v, u32x4 r, u32 sb, u32 lb) {
  glds1<I * LS, I * SS>(v, r, sb, lb);
  if constexpr (I + 1 < N) glds_n<I + 1, N, LS, SS>(v, r, sb, lb);
}
DI void helper_block(Ctx& c, const void* x, const void* wr, const void* wi, const void* wg, const void* wu, int xg, int rb) {
  const int lane = c.lane, w = c.w, t = w & 3, beta = xg * 4 + rb, q = beta & 3, gam = beta >> 2;
  constexpr u32 ONES = 0x3f803f80u, L_FI = 57344, OOB = 0x7ffffff0u;
  u32x2 ep;
  if (w < 4) {
    const int mi2 = beta >> 1;
    const int srow = 1024 * (mi2 >> 3) + 512 * (beta & 1) + 128 * ((mi2 >> 1) & 3) + 16 * (mi2 & 1) + 32 * t + (lane >> 3);
    const u32x4 sx = mk_srd(x, X_BYTES);
    const u32 vx = srow * 896 + (lane & 7) * 16;
    u32x4 xv[2][7];
    ld64(ep, 0u, c.sws);
#pragma unroll
    for (int h = 0; h < 2; ++h) {
      const u32 so = h * 7168;
      ld128<0>(xv[h][0], vx, sx, so); ld128<128>(xv[h][1], vx, sx, so); ld128<256>(xv[h][2], vx, sx, so); ld128<384>(xv[h][3], vx, sx, so);
      ld128<512>(xv[h][4], vx, sx, so); ld128<640>(xv[h][5], vx, sx, so); ld128<768>(xv[h][6], vx, sx, so);
    }
    barrier();
    wait_tag<7>(ep, c.tlo, c.thi);
#pragma unroll
    for (int h = 0; h < 2; ++h) {
      if (h) wait_vm<2>();
#pragma unroll
      for (int i = 0; i < 7; ++i) pin(xv[h][i]);
      float s1a = 0.f, s1b = 0.f, s2a = 0.f, s2b = 0.f;
#pragma unroll
      for (int i = 0; i < 7; ++i)
#pragma unroll
        for (int j = 0; j < 4; j += 2) { s1a = dot2(xv[h][i][j], ONES, s1a); s2a = dot2(xv[h][i][j], xv[h][i][j], s2a); s1b = dot2(xv[h][i][j + 1], ONES, s1b); s2b = dot2(xv[h][i][j + 1], xv[h][i][j + 1], s2b); }
      float s1 = s1a + s1b, s2 = s2a + s2b;
      s1 += __builtin_amdgcn_update_dpp(0.f, s1, 0xB1, 0xf, 0xf, true); s2 += __builtin_amdgcn_update_dpp(0.f, s2, 0xB1, 0xf, 0xf, true);
      s1 += __builtin_amdgcn_update_dpp(0.f, s1, 0x4E, 0xf, 0xf, true); s2 += __builtin_amdgcn_update_dpp(0.f, s2, 0x4E, 0xf, 0xf, true);
      s1 += __builtin_amdgcn_update_dpp(0.f, s1, 0x141, 0xf, 0xf, true); s2 += __builtin_amdgcn_update_dpp(0.f, s2, 0x141, 0xf, 0xf, true);
      const float mean = s1 * (1.f / 448.f);
      const float var = fmaxf(s2 * (1.f / 448.f) - mean * mean, 0.f);
      const float sd = rbf(__builtin_amdgcn_sqrtf(rbf(rbf(var) + 1e-5f))), ri = __builtin_amdgcn_rcpf(sd);
      st128<0>(mk_gran(pk(0.f, -mean), __float_as_uint(sd), c.tlo, c.thi), (lane & 7) ? OOB : (u32)(lane * 2), c.sws, WS_ST + beta * 1024 + t * 256 + h * 128);
      st32(ri * ri, (lane & 7) ? OOB : (u32)(lane >> 1), c.sws, WS_R2 + beta * 256 + t * 64 + h * 32);
    }
    STAMP(1);
  }
  const int job = (xg >> 1) * 4 + rb, mat = job & 1, ni = 7 * (xg & 1) + (job >> 1);
  const u32x4 sm = mk_srd(mat ? wu : wg, job < 14 ? W_BYTES : 0u);
  const int g = lane >> 4, qq = (lane >> 2) & 3, pq = lane & 3;
  const u32 vw = (8 * t + (lane >> 3)) * 3584 + (((lane & 7) ^ (2 * g)) * 16);
  const u32 aw = (4 * g + qq) * 128 + (((2 * t + (pq >> 1)) ^ (2 * ((qq >> 1) | ((g & 1) << 1)))) * 16) + (pq & 1) * 8;
  auto colsum = [&](int hf) {
    u32x4 fr[14];
#pragma unroll
    for (int kk = 0; kk < 14; ++kk) fr[kk] = lds_tr(c.L, aw + kk * 4096, aw + kk * 4096 + 2048);
    SB();
    f32x4 a = {0.f, 0.f, 0.f, 0.f};
#pragma unroll
    for (int kk = 0; kk < 14; ++kk) a = mfma16(u32x4{ONES, ONES, ONES, ONES}, fr[kk], a);
    const float hi = rbf(a[0]);
    st128<0>(mk_gran(__float_as_uint(hi), pk(0.f, a[0] - hi), c.tlo, c.thi), (lane < 16 && job < 14) ? (u32)(lane * 16) : OOB, c.sws, WS_CS + (mat * 1792 + 128 * ni + 64 * hf + 16 * t) * 16);
  };
  if (w >= 4) {
    if (w == 4) *(LAS u32*)(c.L + L_SYNC + lane * 4) = 0u;
    ld64(ep, 0u, c.sws);
    glds_n<0, 14, 4096, 32 * 3584>(vw, sm, ni * 256, t * 1024);
    wait_lgkm0();
    barrier();
    const u32 fo = (16 * q + 256 * gam + 64 * t) * 450;
    glds_n<0, 8, 1024, 1024>(lane * 16, mk_srd(wr, FILT_BYTES), fo, L_FI + t * 16384);
    wait_tag<8>(ep, c.tlo, c.thi);
    lds_sync(Y_ZI, 4);
    colsum(0);
    STAMP(2);
    glds_n<0, 8, 1024, 1024>(lane * 16, mk_srd(wi, FILT_BYTES), fo, L_FI + t * 16384 + 8192);
    lds_sync(Y_ZD, 4);
    glds_n<0, 14, 4096, 32 * 3584>(vw, sm, ni * 256 + 128, t * 1024);
    wait_vm<14>();
  }
  barrier();
  STAMP(3);
  if (w < 4) {
    const int U = (gam >> 1) & 1, gg = 2 * (gam >> 2) + (gam & 1), r = lane & 15;
    const u32 fa = L_FI + r * 450 + (t * 4 + g) * 2;
#pragma unroll
    for (int hb = 0; hb < 3; ++hb) {
      u32x4 o[5];
#pragma unroll
      for (int i = 0; i < 20; ++i) {
        const u32 a = fa + (5 * hb + (i >> 2)) * 32 + (i & 3) * 16384;
        o[i >> 2][i & 3] = (u32) * (LAS unsigned short*)(c.L + a) | ((u32) * (LAS unsigned short*)(c.L + a + 8192) << 16);
      }
#pragma unroll
      for (int i = 0; i < 5; ++i) {
        const int k = (5 * hb + i) * 16 + t * 4 + g;
        st128<0>(o[i], k < 225 ? (u32)(k * 8192 + r * 16) : OOB, c.sws, WS_WT + q * 2048 + U * 1024 + gg * 256);
      }
    }
    STAMP(4);
    wait_vm<0>();
    if (lds_arrive(Y_HA) == 3 && lane == 0) set_flag(c.ws, WS_FLAG_W + beta * 128, c.tlo, c.thi);
    STAMP(5);
  } else {
    wait_vm<0>();
    lds_sync(Y_HB, 4);
    colsum(1);
    STAMP(8);
  }
  if (beta == 0) {
    FftC fc; FftIn fi; FftL fl;
    fft_lane(fl, t, lane);
    fft_gen_all<0>(fc, fl);
    const u32 fo = lane < 16 ? WS_FLAG_A + (lane * 16) * 128 : WS_FLAG_W + ((lane & 7) * 4 + t) * 128;
    poll_flags(u32x2{~c.tlo, 0u}, fo, c.sws, c.tlo, c.thi);
    STAMP(6);
    fft_load(fi, c.sws, t, lane, 0, (w >> 2) * 224);
    wait_vm<0>();
    fft_pin(fi);
#pragma unroll
    for (int i = 0; i < 4; ++i) { if (w >> 2) fi.zr[i >> 1][i & 1] = u32x4{0, 0, 0, 0}; else fi.zi[i >> 1][i & 1] = u32x4{0, 0, 0, 0}; }
    fft_run<1>(fc, fi, c.L, 8 * SLOT, Y_XCH, c.sws, t, w >> 2, lane, 0);
    if (w < 4) v_publish(Y_VD, c.ws, lane, 0, c.tlo, c.thi);
  }
  STAMP(7);
}

DI void src_setup(Ctx& c, int lane) {
  const int q = c.w & 3, g = lane >> 4;
  const int srow = 1024 * (c.mi >> 3) + 128 * ((c.mi >> 1) & 3) + 16 * (c.mi & 1) + 512 * (q >> 1) + 64 * (q & 1) + (lane >> 3);
  c.vx[0] = srow * 896 + (((lane & 7) ^ g) * 16);
  c.vx[1] = (c.vx[0] ^ 64) + (8 * 896 - 1024);
  c.vx[2] = c.vx[0] + (32 * 896 - 2048);
  c.vx[3] = c.vx[1] + (32 * 896 - 2048);
  c.vw[0] = (16 * q + (lane >> 3)) * 3584 + (((lane & 7) ^ (2 * (g & 1))) * 16);
  c.vw[1] = (c.vw[0] ^ 64) + (8 * 3584 - 2048);
  pin(c.vx[0]); pin(c.vx[1]); pin(c.vx[2]); pin(c.vx[3]); pin(c.vw[0]); pin(c.vw[1]);
  SB();
}

__global__ __launch_bounds__(512, 2) void fused_kernel(const void* x, const void* wr, const void* wi, const void* wg, const void* wu, const void* wd, void* out, void* ws) {
  extern __shared__ __attribute__((aligned(16))) char smem[];
  Ctx c;
  c.L = (LAS char*)smem;
  c.ws = (GAS char*)ws;
  const int lane = threadIdx.x & 63, w = __builtin_amdgcn_readfirstlane(threadIdx.x >> 6);
  c.lane = lane; c.w = w;
  STAMP_INIT();
  STAMP(0);
  const int xg = blockIdx.x & 7, rb = blockIdx.x >> 3;
  u64 pc;
  asm volatile("s_getpc_b64 %0" : "=s"(pc));
  u32x4 scode = mk_srd((const void*)(pc & ~127ull), CODE_BYTES);
  u32 zero = 0;
  pins(scode); pins(zero);
  if ((TMASK & 8) && w == 7) touch((rb + 32 * lane) * 128, scode, zero);
  const int rr = rb - 4, r7 = (rr * 37) >> 8, q = w & 3;
  c.mi = 4 * (xg >> 1) + r7; c.nt = rr - 7 * r7; c.ni = 7 * (xg & 1) + c.nt;
#define ROT2 1
#define ROT1 0x26045230
  c.rot = ROT2 == 0 ? 0 : (0x06543210u >> (4 * xg)) & 7;
  if (ROT2 == 2) { c.rot += c.nt; c.rot = c.rot >= 7 ? c.rot - 7 : c.rot; }
  if (ROT2 == 3) c.rot = (xg + 3 * (c.nt >> 1)) % 7;
  if (ROT2 == 4) c.rot = (xg + c.nt + 2 * r7) % 7;
  c.rot1 = ROT1 == 0 ? 0 : (unsigned(ROT1) >> (4 * xg)) & 7;
  c.lb = q * 4096;
  c.sw = c.ni * 256;
  c.sx = c.sg = c.su = u32x4{0u, 0u, 0u, 0u};
  int role = rb < 4 ? 2 : w >> 2;
  if (role == 0) {
    src_setup(c, lane);
    c.sx = mk_srd(x, X_BYTES); c.sg = mk_srd(wg, W_BYTES); c.su = mk_srd((const char*)wu - 1024, W_BYTES + 1024);
    pins(c.sx); pins(c.sg); pins(c.su);
    STAMP(28);
    stage_in<0>(c); stage_in<10>(c);
    __builtin_amdgcn_s_sleep(PSL);
    stage_in<1>(c); stage_in<11>(c);
    STAMP(30);
  }
  asm volatile("" : "+s"(role) : "s"(scode), "s"(zero));
  c.tau = 4 * xg + r7; c.kcol = xg * 28 + rr;
  c.sdn = (128 * (q >> 1) + 32 * (q & 1)) * 896 + c.nt * 128;
  if (role == 0) {
    src_setup(c, pv(lane));
    service_waves(c, wr, wi, wd, out, ws);
  } else if (role == 1) {
    compute_waves(c, x, wd, out, ws);
  } else {
    c.sws = mk_srd(ws, WS_END);
    helper_block(c, x, wr, wi, wg, wu, xg, rb);
  }
  STAMP_DUMP();
}

extern "C" void kernel_launch(void* const* d_in, const int* in_sizes, int n_in, void* d_out, int out_size, void* d_ws, size_t ws_size, hipStream_t stream) {
  (void)in_sizes; (void)n_in; (void)out_size; (void)ws_size;
  (void)hipFuncSetAttribute((const void*)fused_kernel, hipFuncAttributeMaxDynamicSharedMemorySize, LDS_BYTES);
  fused_kernel<<<256, 512, LDS_BYTES, stream>>>(d_in[0], d_in[1], d_in[2], d_in[4], d_in[5], d_in[6], d_out, (char*)d_ws + ((size_t)(WSOFF) << 20));
}
```
